```python
import torch

def _softmax(x, axis=-1):
    return torch.softmax(x, dim=axis)
def _gelu(x):
    return torch.nn.functional.gelu(x, approximate="tanh")
def _silu(x):
    return torch.nn.functional.silu(x)
def _sigmoid(x):
    return torch.sigmoid(x)
def _layernorm(x, axis=-1, eps=1e-5):
    m = x.mean(dim=axis, keepdim=True)
    v = x.var(dim=axis, keepdim=True, unbiased=False)
    return (x - m) / torch.sqrt(v + eps)
def _scatter_add(tgt, idx, src, axis=0):
    return tgt.clone().index_add_(axis, idx, src)
def _segment_sum(src, seg, n):
    out = torch.zeros((n,) + src.shape[1:], dtype=src.dtype)
    return out.index_add_(0, seg, src)
def _pad(x, pw):
    flat = tuple(v for pair in reversed(pw) for v in pair)
    return torch.nn.functional.pad(x, flat)

kernel_name = 'standalone__gqa_softmax__near_ridge'

def setup_inputs():

    _w = (lambda a: (lambda t: t.to(torch.bfloat16) if t.is_floating_point() else t)(torch.as_tensor(a)))
    B, Hq, Hkv, Sq, Sk, D = 64, 32, 8, 256, 256, 128
    return {
        "q": _w((lambda *s: torch.randn(*s, dtype=torch.bfloat16) * 0.8)(B, Hq, Sq, D)),
        "k": _w((lambda *s: torch.randn(*s, dtype=torch.bfloat16) * 0.8)(B, Hkv, Sk, D)),
        "v": _w((lambda *s: torch.randn(*s, dtype=torch.bfloat16) * 0.05)(B, Hkv, Sk, D)),
    }

def reference(q, k, v):
    B, Hq, Sq, D = q.shape
    Hkv = k.shape[1]
    rep = Hq // Hkv
    k = (lambda x, n, axis: torch.repeat_interleave(x, n, dim=axis))(k, rep, axis=1)
    v = (lambda x, n, axis: torch.repeat_interleave(x, n, dim=axis))(v, rep, axis=1)
    scale = D ** -0.5
    scores = torch.einsum("bhqd,bhkd->bhqk", q, k) * scale
    p = _softmax(scores, axis=-1)
    return torch.einsum("bhqk,bhkd->bhqd", p, v)
```

```cpp
#include <hip/hip_runtime.h>
#include <hip/hip_bf16.h>

using bf16 = __hip_bfloat16;
constexpr int B_ = 64, HQ = 32, HKV = 8, SQ = 256, SK = 256, D = 128, G = HQ / HKV;
constexpr float SCALE = 0.088388347648318440f;

using bf16x8 = __attribute__((ext_vector_type(8))) short;
using s16x4  = __attribute__((ext_vector_type(4))) short;
using f32x16 = __attribute__((ext_vector_type(16))) float;
using u32x4  = __attribute__((ext_vector_type(4))) unsigned;

#define KSWZ(row, colB) ((row) * 256 + ((colB) ^ (((row) & 15) << 4)))
#define SBAR() __builtin_amdgcn_sched_barrier(0)

__device__ __forceinline__ unsigned cvtpk(float lo, float hi) {
  unsigned r; asm volatile("v_cvt_pk_bf16_f32 %0, %1, %2" : "=v"(r) : "v"(lo), "v"(hi)); return r;
}
__device__ __forceinline__ int v_st(int k, int c) {
  const int kk = (k & ~0xC) | ((k & 4) << 1) | ((k & 8) >> 1);
  return ((kk >> 3) * 4 + (c >> 5)) * 512 + ((kk & 7) * 32 + (c & 31)) * 2;
}
__device__ __forceinline__ int v_rd_base(int lane) {
  return ((lane & 3) << 3) | (((lane >> 2) & 3) << 6) | (((lane >> 4) & 1) << 5) | (((lane >> 5) & 1) << 8);
}
constexpr int v_rd_off(int d0, int ks, int half) { return d0 * 512 + ks * 4096 + half * 2048; }
template <int OFF> __device__ __forceinline__ s16x4 tr_read(int vb) {
  s16x4 r; asm volatile("ds_read_b64_tr_b16 %0, %1 offset:%2" : "=&v"(r) : "v"(vb), "i"(OFF) : "memory"); return r;
}

template <int KS0>
__device__ __forceinline__ void pv4(f32x16& o0, f32x16& o1, f32x16& o2, f32x16& o3, int vb, const bf16x8* pa) {
  s16x4 l[2][4], h[2][4];
#define RD(ks, d0) l[ks][d0] = tr_read<v_rd_off(d0, KS0 + ks, 0)>(vb); h[ks][d0] = tr_read<v_rd_off(d0, KS0 + ks, 1)>(vb);
  RD(0,0) RD(0,1) RD(0,2) RD(0,3) RD(1,0) RD(1,1) RD(1,2) RD(1,3)
#undef RD
  asm volatile("s_waitcnt lgkmcnt(0)" ::: "memory"); SBAR();
#define PK(L, H) (bf16x8){L[0], L[1], L[2], L[3], H[0], H[1], H[2], H[3]}
  o0 = __builtin_amdgcn_mfma_f32_32x32x16_bf16(PK(l[0][0], h[0][0]), pa[KS0], o0, 0, 0, 0);
  o1 = __builtin_amdgcn_mfma_f32_32x32x16_bf16(PK(l[0][1], h[0][1]), pa[KS0], o1, 0, 0, 0);
  o2 = __builtin_amdgcn_mfma_f32_32x32x16_bf16(PK(l[0][2], h[0][2]), pa[KS0], o2, 0, 0, 0);
  o3 = __builtin_amdgcn_mfma_f32_32x32x16_bf16(PK(l[0][3], h[0][3]), pa[KS0], o3, 0, 0, 0);
  o0 = __builtin_amdgcn_mfma_f32_32x32x16_bf16(PK(l[1][0], h[1][0]), pa[KS0 + 1], o0, 0, 0, 0);
  o1 = __builtin_amdgcn_mfma_f32_32x32x16_bf16(PK(l[1][1], h[1][1]), pa[KS0 + 1], o1, 0, 0, 0);
  o2 = __builtin_amdgcn_mfma_f32_32x32x16_bf16(PK(l[1][2], h[1][2]), pa[KS0 + 1], o2, 0, 0, 0);
  o3 = __builtin_amdgcn_mfma_f32_32x32x16_bf16(PK(l[1][3], h[1][3]), pa[KS0 + 1], o3, 0, 0, 0);
  asm volatile("" : "+v"(o0), "+v"(o1), "+v"(o2), "+v"(o3));
  SBAR();
#undef PK
}

__global__ __launch_bounds__(512, 2)
void gqa_attn_kernel(const bf16* __restrict__ Q, const bf16* __restrict__ K, const bf16* __restrict__ V,
                     bf16* __restrict__ O) {
  extern __shared__ __attribute__((aligned(16))) char lds[];
  char* K_lds = lds;
  char* V_lds = lds + 65536;
  const int tid = threadIdx.x, wid = tid >> 6, lane = tid & 63, r32 = lane & 31, hi = lane >> 5;
  const int bk = blockIdx.x;
  const bf16* Kh = K + (long)bk * SK * D;
  const bf16* Vh = V + (long)bk * SK * D;
  const long qbase = (long)bk * G * SQ * D;

  {
    const int sr = tid >> 4, sc = (tid & 15) * 8;
    bf16x8 kr[8], vr[8];
#pragma unroll
    for (int i = 0; i < 8; ++i) kr[i] = *reinterpret_cast<const bf16x8*>(Kh + (long)(i * 32 + sr) * D + sc);
#pragma unroll
    for (int i = 0; i < 8; ++i) vr[i] = *reinterpret_cast<const bf16x8*>(Vh + (long)(i * 32 + sr) * D + sc);
#pragma unroll
    for (int i = 0; i < 8; ++i) *reinterpret_cast<bf16x8*>(K_lds + KSWZ(i * 32 + sr, sc * 2)) = kr[i];
#pragma unroll
    for (int i = 0; i < 8; ++i) *reinterpret_cast<bf16x8*>(V_lds + v_st(i * 32 + sr, sc)) = vr[i];
  }
  bf16x8 qr[8];
  {
    const bf16* Qw = Q + qbase + (long)(wid * 32 + r32) * D + hi * 8;
#pragma unroll
    for (int d0 = 0; d0 < 8; ++d0) qr[d0] = *reinterpret_cast<const bf16x8*>(Qw + d0 * 16);
  }
  __syncthreads();
  const int vb = (int)(uintptr_t)V_lds + v_rd_base(lane);
  constexpr float C = SCALE * 1.4426950408889634f;

  for (int g = 0; g < G; ++g) {
    f32x16 s[8];
#pragma unroll
    for (int kb = 0; kb < 8; ++kb) {
      s[kb] = f32x16{};
#pragma unroll
      for (int d0 = 0; d0 < 8; ++d0) {
        bf16x8 kf = *reinterpret_cast<const bf16x8*>(K_lds + KSWZ(kb * 32 + r32, (d0 * 16 + hi * 8) * 2));
        s[kb] = __builtin_amdgcn_mfma_f32_32x32x16_bf16(kf, qr[d0], s[kb], 0, 0, 0);
      }
      SBAR();
    }
    if (g + 1 < G) {
      const bf16* Qw = Q + qbase + (long)((g + 1) * SQ + wid * 32 + r32) * D + hi * 8;
#pragma unroll
      for (int d0 = 0; d0 < 8; ++d0) qr[d0] = *reinterpret_cast<const bf16x8*>(Qw + d0 * 16);
    }
    float m = s[0][0];
#pragma unroll
    for (int kb = 0; kb < 8; ++kb)
#pragma unroll
      for (int r = 0; r < 16; ++r) m = fmaxf(m, s[kb][r]);
    { auto rr = __builtin_amdgcn_permlane32_swap(__float_as_uint(m), __float_as_uint(m), false, false);
      m = fmaxf(__uint_as_float(rr[0]), __uint_as_float(rr[1])); }
    const float mC = -m * C;
    float l = 0.f;
    bf16x8 pa[16];
#define PK4(P, BASE, OUT) do { unsigned a0 = cvtpk(P[BASE + 0], P[BASE + 1]), a1 = cvtpk(P[BASE + 2], P[BASE + 3]);   \
    unsigned b0 = cvtpk(P[BASE + 4], P[BASE + 5]), b1 = cvtpk(P[BASE + 6], P[BASE + 7]);                              \
    auto r0 = __builtin_amdgcn_permlane32_swap(a0, b0, false, false); auto r1 = __builtin_amdgcn_permlane32_swap(a1, b1, false, false); \
    u32x4 w = {r0[0], r1[0], r0[1], r1[1]}; OUT = *reinterpret_cast<bf16x8*>(&w); } while (0)
#pragma unroll
    for (int kb = 0; kb < 8; ++kb) {
      float pp[16];
#pragma unroll
      for (int r = 0; r < 16; ++r) { pp[r] = __builtin_amdgcn_exp2f(fmaf(s[kb][r], C, mC)); l += pp[r]; }
      PK4(pp, 0, pa[2 * kb]); PK4(pp, 8, pa[2 * kb + 1]);
      SBAR();
    }
#undef PK4
    { auto rr = __builtin_amdgcn_permlane32_swap(__float_as_uint(l), __float_as_uint(l), false, false);
      l = __uint_as_float(rr[0]) + __uint_as_float(rr[1]); }
    f32x16 o0 = {}, o1 = {}, o2 = {}, o3 = {};
    pv4<0>(o0, o1, o2, o3, vb, pa);  pv4<2>(o0, o1, o2, o3, vb, pa);  pv4<4>(o0, o1, o2, o3, vb, pa);  pv4<6>(o0, o1, o2, o3, vb, pa);
    pv4<8>(o0, o1, o2, o3, vb, pa);  pv4<10>(o0, o1, o2, o3, vb, pa); pv4<12>(o0, o1, o2, o3, vb, pa); pv4<14>(o0, o1, o2, o3, vb, pa);
    const float rl = __builtin_amdgcn_rcpf(l);
    bf16* Ow = O + qbase + (long)(g * SQ + wid * 32 + r32) * D;
#define STORE(OB, D0) do {                                                                                   \
      _Pragma("unroll") for (int gg = 0; gg < 4; gg += 2) {                                                  \
        unsigned ax = cvtpk(OB[4 * gg + 0] * rl, OB[4 * gg + 1] * rl), ay = cvtpk(OB[4 * gg + 2] * rl, OB[4 * gg + 3] * rl); \
        unsigned bx = cvtpk(OB[4 * gg + 4] * rl, OB[4 * gg + 5] * rl), by = cvtpk(OB[4 * gg + 6] * rl, OB[4 * gg + 7] * rl); \
        auto rx = __builtin_amdgcn_permlane32_swap(ax, bx, false, false);                                    \
        auto ry = __builtin_amdgcn_permlane32_swap(ay, by, false, false);                                    \
        u32x4 w = {rx[0], ry[0], rx[1], ry[1]};                                                              \
        *reinterpret_cast<u32x4*>(Ow + (D0) * 32 + gg * 8 + hi * 8) = w;                                     \
      } } while (0)
    STORE(o0, 0); STORE(o1, 1); STORE(o2, 2); STORE(o3, 3);
#undef STORE
  }
}

extern "C" void kernel_launch(void* const* d_in, const int* in_sizes, int n_in,
                              void* d_out, int out_size, void* d_ws, size_t ws_size,
                              hipStream_t stream) {
  (void)in_sizes; (void)n_in; (void)out_size; (void)d_ws; (void)ws_size;
  hipFuncSetAttribute((const void*)gqa_attn_kernel, hipFuncAttributeMaxDynamicSharedMemorySize, 131072);
  hipLaunchKernelGGL(gqa_attn_kernel, dim3(B_ * HKV), dim3(512), 131072, stream,
                     (const bf16*)d_in[0], (const bf16*)d_in[1], (const bf16*)d_in[2], (bf16*)d_out);
}
```

```cpp
#include <hip/hip_runtime.h>
#include <hip/hip_bf16.h>

using bf16 = __hip_bfloat16;
constexpr int B_ = 64, HQ = 32, HKV = 8, SQ = 256, SK = 256, D = 128, G = HQ / HKV;
constexpr float SCALE = 0.088388347648318440f;

using bf16x8 = __attribute__((ext_vector_type(8))) short;
using s16x4  = __attribute__((ext_vector_type(4))) short;
using f32x16 = __attribute__((ext_vector_type(16))) float;
using u32x4  = __attribute__((ext_vector_type(4))) unsigned;

#define KSWZ(row, colB) ((row) * 256 + ((colB) ^ (((row) & 15) << 4)))
#define SBAR() __builtin_amdgcn_sched_barrier(0)

__device__ __forceinline__ unsigned cvtpk(float lo, float hi) {
  unsigned r; asm volatile("v_cvt_pk_bf16_f32 %0, %1, %2" : "=v"(r) : "v"(lo), "v"(hi)); return r;
}
__device__ __forceinline__ int v_st(int k, int c) {
  const int kk = (k & ~0xC) | ((k & 4) << 1) | ((k & 8) >> 1);
  return ((kk >> 3) * 4 + (c >> 5)) * 512 + ((kk & 7) * 32 + (c & 31)) * 2;
}
__device__ __forceinline__ int v_rd_base(int lane) {
  return ((lane & 3) << 3) | (((lane >> 2) & 3) << 6) | (((lane >> 4) & 1) << 5) | (((lane >> 5) & 1) << 8);
}
constexpr int v_rd_off(int d0, int ks, int half) { return d0 * 512 + ks * 4096 + half * 2048; }
template <int OFF> __device__ __forceinline__ s16x4 tr_read(int vb) {
  s16x4 r; asm volatile("ds_read_b64_tr_b16 %0, %1 offset:%2" : "=&v"(r) : "v"(vb), "i"(OFF) : "memory"); return r;
}

struct VFrag { s16x4 l[4], h[4]; };
template <int KS> __device__ __forceinline__ void v_read(VFrag& f, int vb) {
  f.l[0] = tr_read<v_rd_off(0, KS, 0)>(vb); f.h[0] = tr_read<v_rd_off(0, KS, 1)>(vb);
  f.l[1] = tr_read<v_rd_off(1, KS, 0)>(vb); f.h[1] = tr_read<v_rd_off(1, KS, 1)>(vb);
  f.l[2] = tr_read<v_rd_off(2, KS, 0)>(vb); f.h[2] = tr_read<v_rd_off(2, KS, 1)>(vb);
  f.l[3] = tr_read<v_rd_off(3, KS, 0)>(vb); f.h[3] = tr_read<v_rd_off(3, KS, 1)>(vb);
}
#define PK(L, H) (bf16x8){L[0], L[1], L[2], L[3], H[0], H[1], H[2], H[3]}
__device__ __forceinline__ void v_mma(f32x16& o0, f32x16& o1, f32x16& o2, f32x16& o3, const VFrag& f, bf16x8 p) {
  o0 = __builtin_amdgcn_mfma_f32_32x32x16_bf16(PK(f.l[0], f.h[0]), p, o0, 0, 0, 0);
  o1 = __builtin_amdgcn_mfma_f32_32x32x16_bf16(PK(f.l[1], f.h[1]), p, o1, 0, 0, 0);
  o2 = __builtin_amdgcn_mfma_f32_32x32x16_bf16(PK(f.l[2], f.h[2]), p, o2, 0, 0, 0);
  o3 = __builtin_amdgcn_mfma_f32_32x32x16_bf16(PK(f.l[3], f.h[3]), p, o3, 0, 0, 0);
  asm volatile("" : "+v"(o0), "+v"(o1), "+v"(o2), "+v"(o3));
}
template <int KS> __device__ __forceinline__ void pv_step(f32x16& o0, f32x16& o1, f32x16& o2, f32x16& o3,
                                                       VFrag& fa, VFrag& fb, int vb, bf16x8 p) {
  if constexpr (KS + 1 < 16) { v_read<KS + 1>(fb, vb); asm volatile("s_waitcnt lgkmcnt(8)" ::: "memory"); }
  else { asm volatile("s_waitcnt lgkmcnt(0)" ::: "memory"); }
  SBAR();
  v_mma(o0, o1, o2, o3, fa, p);
  SBAR();
}

__global__ __launch_bounds__(512, 2)
void gqa_attn_kernel(const bf16* __restrict__ Q, const bf16* __restrict__ K, const bf16* __restrict__ V,
                     bf16* __restrict__ O) {
  extern __shared__ __attribute__((aligned(16))) char lds[];
  char* K_lds = lds;
  char* V_lds = lds + 65536;
  const int tid = threadIdx.x, wid = tid >> 6, lane = tid & 63, r32 = lane & 31, hi = lane >> 5;
  const int bk = blockIdx.x;
  const bf16* Kh = K + (long)bk * SK * D;
  const bf16* Vh = V + (long)bk * SK * D;
  const long qbase = (long)bk * G * SQ * D;

  {
    const int sr = tid >> 4, sc = (tid & 15) * 8;
    bf16x8 kr[8], vr[8];
#pragma unroll
    for (int i = 0; i < 8; ++i) kr[i] = *reinterpret_cast<const bf16x8*>(Kh + (long)(i * 32 + sr) * D + sc);
#pragma unroll
    for (int i = 0; i < 8; ++i) vr[i] = *reinterpret_cast<const bf16x8*>(Vh + (long)(i * 32 + sr) * D + sc);
#pragma unroll
    for (int i = 0; i < 8; ++i) *reinterpret_cast<bf16x8*>(K_lds + KSWZ(i * 32 + sr, sc * 2)) = kr[i];
#pragma unroll
    for (int i = 0; i < 8; ++i) *reinterpret_cast<bf16x8*>(V_lds + v_st(i * 32 + sr, sc)) = vr[i];
  }
  bf16x8 qr[8];
  {
    const bf16* Qw = Q + qbase + (long)(wid * 32 + r32) * D + hi * 8;
#pragma unroll
    for (int d0 = 0; d0 < 8; ++d0) qr[d0] = *reinterpret_cast<const bf16x8*>(Qw + d0 * 16);
  }
  __syncthreads();
  const int vb = (int)(uintptr_t)V_lds + v_rd_base(lane);
  constexpr float C = SCALE * 1.4426950408889634f;

  for (int g = 0; g < G; ++g) {
    f32x16 s[8];
#pragma unroll
    for (int kb = 0; kb < 8; ++kb) {
      s[kb] = f32x16{};
#pragma unroll
      for (int d0 = 0; d0 < 8; ++d0) {
        bf16x8 kf = *reinterpret_cast<const bf16x8*>(K_lds + KSWZ(kb * 32 + r32, (d0 * 16 + hi * 8) * 2));
        s[kb] = __builtin_amdgcn_mfma_f32_32x32x16_bf16(kf, qr[d0], s[kb], 0, 0, 0);
      }
    }
    if (g + 1 < G) {
      const bf16* Qw = Q + qbase + (long)((g + 1) * SQ + wid * 32 + r32) * D + hi * 8;
#pragma unroll
      for (int d0 = 0; d0 < 8; ++d0) qr[d0] = *reinterpret_cast<const bf16x8*>(Qw + d0 * 16);
    }
    float m = s[0][0];
#pragma unroll
    for (int kb = 0; kb < 8; ++kb)
#pragma unroll
      for (int r = 0; r < 16; ++r) m = fmaxf(m, s[kb][r]);
    { auto rr = __builtin_amdgcn_permlane32_swap(__float_as_uint(m), __float_as_uint(m), false, false);
      m = fmaxf(__uint_as_float(rr[0]), __uint_as_float(rr[1])); }
    const float mC = -m * C;
    float l = 0.f;
    bf16x8 pa[16];
#define PK4(P, BASE, OUT) do { unsigned a0 = cvtpk(P[BASE + 0], P[BASE + 1]), a1 = cvtpk(P[BASE + 2], P[BASE + 3]);   \
    unsigned b0 = cvtpk(P[BASE + 4], P[BASE + 5]), b1 = cvtpk(P[BASE + 6], P[BASE + 7]);                              \
    auto r0 = __builtin_amdgcn_permlane32_swap(a0, b0, false, false); auto r1 = __builtin_amdgcn_permlane32_swap(a1, b1, false, false); \
    u32x4 w = {r0[0], r1[0], r0[1], r1[1]}; OUT = *reinterpret_cast<bf16x8*>(&w); } while (0)
#pragma unroll
    for (int kb = 0; kb < 8; ++kb) {
      float pp[16];
#pragma unroll
      for (int r = 0; r < 16; ++r) { pp[r] = __builtin_amdgcn_exp2f(fmaf(s[kb][r], C, mC)); l += pp[r]; }
      PK4(pp, 0, pa[2 * kb]); PK4(pp, 8, pa[2 * kb + 1]);
      SBAR();
    }
#undef PK4
    { auto rr = __builtin_amdgcn_permlane32_swap(__float_as_uint(l), __float_as_uint(l), false, false);
      l = __uint_as_float(rr[0]) + __uint_as_float(rr[1]); }
    f32x16 o0 = {}, o1 = {}, o2 = {}, o3 = {};
    {
      VFrag f0, f1;
      v_read<0>(f0, vb);
      pv_step<0>(o0, o1, o2, o3, f0, f1, vb, pa[0]);
      pv_step<1>(o0, o1, o2, o3, f1, f0, vb, pa[1]);
      pv_step<2>(o0, o1, o2, o3, f0, f1, vb, pa[2]);
      pv_step<3>(o0, o1, o2, o3, f1, f0, vb, pa[3]);
      pv_step<4>(o0, o1, o2, o3, f0, f1, vb, pa[4]);
      pv_step<5>(o0, o1, o2, o3, f1, f0, vb, pa[5]);
      pv_step<6>(o0, o1, o2, o3, f0, f1, vb, pa[6]);
      pv_step<7>(o0, o1, o2, o3, f1, f0, vb, pa[7]);
      pv_step<8>(o0, o1, o2, o3, f0, f1, vb, pa[8]);
      pv_step<9>(o0, o1, o2, o3, f1, f0, vb, pa[9]);
      pv_step<10>(o0, o1, o2, o3, f0, f1, vb, pa[10]);
      pv_step<11>(o0, o1, o2, o3, f1, f0, vb, pa[11]);
      pv_step<12>(o0, o1, o2, o3, f0, f1, vb, pa[12]);
      pv_step<13>(o0, o1, o2, o3, f1, f0, vb, pa[13]);
      pv_step<14>(o0, o1, o2, o3, f0, f1, vb, pa[14]);
      pv_step<15>(o0, o1, o2, o3, f1, f0, vb, pa[15]);
    }
    const float rl = __builtin_amdgcn_rcpf(l);
    bf16* Ow = O + qbase + (long)(g * SQ + wid * 32 + r32) * D;
#define STORE(OB, D0) do {                                                                                   \
      _Pragma("unroll") for (int gg = 0; gg < 4; gg += 2) {                                                  \
        unsigned ax = cvtpk(OB[4 * gg + 0] * rl, OB[4 * gg + 1] * rl), ay = cvtpk(OB[4 * gg + 2] * rl, OB[4 * gg + 3] * rl); \
        unsigned bx = cvtpk(OB[4 * gg + 4] * rl, OB[4 * gg + 5] * rl), by = cvtpk(OB[4 * gg + 6] * rl, OB[4 * gg + 7] * rl); \
        auto rx = __builtin_amdgcn_permlane32_swap(ax, bx, false, false);                                    \
        auto ry = __builtin_amdgcn_permlane32_swap(ay, by, false, false);                                    \
        u32x4 w = {rx[0], ry[0], rx[1], ry[1]};                                                              \
        *reinterpret_cast<u32x4*>(Ow + (D0) * 32 + gg * 8 + hi * 8) = w;                                     \
      } } while (0)
    STORE(o0, 0); STORE(o1, 1); STORE(o2, 2); STORE(o3, 3);
#undef STORE
  }
}

extern "C" void kernel_launch(void* const* d_in, const int* in_sizes, int n_in,
                              void* d_out, int out_size, void* d_ws, size_t ws_size,
                              hipStream_t stream) {
  (void)in_sizes; (void)n_in; (void)out_size; (void)d_ws; (void)ws_size;
  hipFuncSetAttribute((const void*)gqa_attn_kernel, hipFuncAttributeMaxDynamicSharedMemorySize, 131072);
  hipLaunchKernelGGL(gqa_attn_kernel, dim3(B_ * HKV), dim3(512), 131072, stream,
                     (const bf16*)d_in[0], (const bf16*)d_in[1], (const bf16*)d_in[2], (bf16*)d_out);
}
```

```cpp
#include <hip/hip_runtime.h>
#include <hip/hip_bf16.h>
#define STAMP(i)

using bf16 = __hip_bfloat16;
constexpr int B_ = 64, HQ = 32, HKV = 8, SQ = 256, SK = 256, D = 128, G = HQ / HKV;
constexpr float SCALE = 0.088388347648318440f;

using bf16x8 = __attribute__((ext_vector_type(8))) short;
using s16x4  = __attribute__((ext_vector_type(4))) short;
using f32x16 = __attribute__((ext_vector_type(16))) float;
using u32x4  = __attribute__((ext_vector_type(4))) unsigned;

#define KSWZ(row, colB) ((row) * 256 + ((colB) ^ (((row) & 15) << 4)))
#define SBAR() __builtin_amdgcn_sched_barrier(0)

__device__ __forceinline__ unsigned cvtpk(float lo, float hi) {
  unsigned r; asm volatile("v_cvt_pk_bf16_f32 %0, %1, %2" : "=v"(r) : "v"(lo), "v"(hi)); return r;
}
__device__ __forceinline__ int v_st(int k, int c) {
  const int kk = (k & ~0xC) | ((k & 4) << 1) | ((k & 8) >> 1);
  return ((kk >> 3) * 4 + (c >> 5)) * 512 + ((kk & 7) * 32 + (c & 31)) * 2;
}
__device__ __forceinline__ int v_rd_base(int lane) {
  return ((lane & 3) << 3) | (((lane >> 2) & 3) << 6) | (((lane >> 4) & 1) << 5) | (((lane >> 5) & 1) << 8);
}
constexpr int v_rd_off(int d0, int ks, int half) { return d0 * 512 + ks * 4096 + half * 2048; }
template <int OFF> __device__ __forceinline__ s16x4 tr_read(int vb) {
  s16x4 r; asm volatile("ds_read_b64_tr_b16 %0, %1 offset:%2" : "=&v"(r) : "v"(vb), "i"(OFF) : "memory"); return r;
}

struct VFrag { s16x4 l[4], h[4]; };
template <int KS> __device__ __forceinline__ void v_read(VFrag& f, int vb) {
  f.l[0] = tr_read<v_rd_off(0, KS, 0)>(vb); f.h[0] = tr_read<v_rd_off(0, KS, 1)>(vb);
  f.l[1] = tr_read<v_rd_off(1, KS, 0)>(vb); f.h[1] = tr_read<v_rd_off(1, KS, 1)>(vb);
  f.l[2] = tr_read<v_rd_off(2, KS, 0)>(vb); f.h[2] = tr_read<v_rd_off(2, KS, 1)>(vb);
  f.l[3] = tr_read<v_rd_off(3, KS, 0)>(vb); f.h[3] = tr_read<v_rd_off(3, KS, 1)>(vb);
}
#define PK(L, H) (bf16x8){L[0], L[1], L[2], L[3], H[0], H[1], H[2], H[3]}
__device__ __forceinline__ void v_mma(f32x16& o0, f32x16& o1, f32x16& o2, f32x16& o3, const VFrag& f, bf16x8 p) {
  o0 = __builtin_amdgcn_mfma_f32_32x32x16_bf16(PK(f.l[0], f.h[0]), p, o0, 0, 0, 0);
  o1 = __builtin_amdgcn_mfma_f32_32x32x16_bf16(PK(f.l[1], f.h[1]), p, o1, 0, 0, 0);
  o2 = __builtin_amdgcn_mfma_f32_32x32x16_bf16(PK(f.l[2], f.h[2]), p, o2, 0, 0, 0);
  o3 = __builtin_amdgcn_mfma_f32_32x32x16_bf16(PK(f.l[3], f.h[3]), p, o3, 0, 0, 0);
  asm volatile("" : "+v"(o0), "+v"(o1), "+v"(o2), "+v"(o3));
}
template <int KS> __device__ __forceinline__ void pv_step(f32x16& o0, f32x16& o1, f32x16& o2, f32x16& o3,
                                                       VFrag& fa, VFrag& fb, int vb, bf16x8 p) {
  if constexpr (KS + 1 < 16) { v_read<KS + 1>(fb, vb); asm volatile("s_waitcnt lgkmcnt(8)" ::: "memory"); }
  else { asm volatile("s_waitcnt lgkmcnt(0)" ::: "memory"); }
  SBAR();
  v_mma(o0, o1, o2, o3, fa, p);
  SBAR();
}

__global__ __launch_bounds__(512, 2)
void gqa_attn_kernel(const bf16* __restrict__ Q, const bf16* __restrict__ K, const bf16* __restrict__ V,
                     bf16* __restrict__ O, unsigned long long* ts) {
  extern __shared__ __attribute__((aligned(16))) char lds[];
  char* K_lds = lds;
  char* V_lds = lds + 65536;
  const int tid = threadIdx.x, wid = tid >> 6, lane = tid & 63, r32 = lane & 31, hi = lane >> 5;
  const int sr = tid >> 4, sc = (tid & 15) * 8;
  const int vb = (int)(uintptr_t)V_lds + v_rd_base(lane);
  constexpr float C = SCALE * 1.4426950408889634f;
  constexpr int NITEM = 2;
  STAMP(0);
  bf16x8 qr[8];
#define DMA_K(bk) do { int lane = tid & 63; asm volatile("" : "+v"(lane)); const bf16* Kh = K + (long)(bk) * SK * D;                                               \
    _Pragma("unroll") for (int i = 0; i < 8; ++i) { const int pc = wid * 8 + i, row = pc * 4 + (lane >> 4);     \
      __builtin_amdgcn_global_load_lds((const void*)(Kh + row * D + (((lane & 15) ^ (row & 15)) * 8)),          \
        (__attribute__((address_space(3))) void*)(K_lds + pc * 1024), 16, 0, 0); } } while (0)
#define DMA_V(bk) do { int lane = tid & 63; asm volatile("" : "+v"(lane)); const bf16* Vh = V + (long)(bk) * SK * D;                                               \
    _Pragma("unroll") for (int i = 0; i < 8; ++i) { const int pc = wid * 8 + i, st = pc * 2 + (lane >> 5);      \
      const int kk = (st >> 2) * 8 + ((lane & 31) >> 2), k = (kk & ~0xC) | ((kk & 4) << 1) | ((kk & 8) >> 1);   \
      __builtin_amdgcn_global_load_lds((const void*)(Vh + k * D + (st & 3) * 32 + (lane & 3) * 8),               \
        (__attribute__((address_space(3))) void*)(V_lds + pc * 1024), 16, 0, 0); } } while (0)
#define LOAD_Q(bk, g) do { const bf16* Qw = Q + ((long)(bk) * G * SQ + (g) * SQ + wid * 32 + r32) * D + hi * 8;  \
    _Pragma("unroll") for (int d0 = 0; d0 < 8; ++d0) qr[d0] = *reinterpret_cast<const bf16x8*>(Qw + d0 * 16); } while (0)

  const int bk0 = blockIdx.x;
  DMA_K(bk0); LOAD_Q(bk0, 0);
  __syncthreads();
  STAMP(1);

  for (int item = 0; item < NITEM; ++item) {
    const int bk = bk0 + item * 256;
    const bool more = item + 1 < NITEM;
    DMA_V(bk);
#pragma unroll 1
    for (int g = 0; g < G; ++g) {
      f32x16 s[8];
#pragma unroll
      for (int kb = 0; kb < 8; ++kb) {
        s[kb] = f32x16{};
#pragma unroll
        for (int d0 = 0; d0 < 8; ++d0) {
          bf16x8 kf = *reinterpret_cast<const bf16x8*>(K_lds + KSWZ(kb * 32 + r32, (d0 * 16 + hi * 8) * 2));
          s[kb] = __builtin_amdgcn_mfma_f32_32x32x16_bf16(kf, qr[d0], s[kb], 0, 0, 0);
        }
      }
      STAMP(2 + (item * 4 + g) * 4);
      if (g + 1 < G) { LOAD_Q(bk, g + 1); }
      else if (more) { LOAD_Q(bk + 256, 0); }
      float m = s[0][0];
#pragma unroll
      for (int kb = 0; kb < 8; ++kb)
#pragma unroll
        for (int r = 0; r < 16; ++r) m = fmaxf(m, s[kb][r]);
      { auto rr = __builtin_amdgcn_permlane32_swap(__float_as_uint(m), __float_as_uint(m), false, false);
        m = fmaxf(__uint_as_float(rr[0]), __uint_as_float(rr[1])); }
      const float mC = -m * C;
      float l = 0.f;
      bf16x8 pa[16];
#define PK4(P, BASE, OUT) do { unsigned a0 = cvtpk(P[BASE + 0], P[BASE + 1]), a1 = cvtpk(P[BASE + 2], P[BASE + 3]);   \
    unsigned b0 = cvtpk(P[BASE + 4], P[BASE + 5]), b1 = cvtpk(P[BASE + 6], P[BASE + 7]);                              \
    auto r0 = __builtin_amdgcn_permlane32_swap(a0, b0, false, false); auto r1 = __builtin_amdgcn_permlane32_swap(a1, b1, false, false); \
    u32x4 w = {r0[0], r1[0], r0[1], r1[1]}; OUT = *reinterpret_cast<bf16x8*>(&w); } while (0)
#pragma unroll
      for (int kb = 0; kb < 8; ++kb) {
        float pp[16];
#pragma unroll
        for (int r = 0; r < 16; ++r) { pp[r] = __builtin_amdgcn_exp2f(fmaf(s[kb][r], C, mC)); l += pp[r]; }
        PK4(pp, 0, pa[2 * kb]); PK4(pp, 8, pa[2 * kb + 1]);
        SBAR();
      }
#undef PK4
      { auto rr = __builtin_amdgcn_permlane32_swap(__float_as_uint(l), __float_as_uint(l), false, false);
        l = __uint_as_float(rr[0]) + __uint_as_float(rr[1]); }
      if (g == 0) { asm volatile("s_waitcnt vmcnt(0)" ::: "memory"); __syncthreads(); }
      if (g == G - 1 && more) { __syncthreads(); DMA_K(bk + 256); }
      STAMP(3 + (item * 4 + g) * 4);
      f32x16 o0 = {}, o1 = {}, o2 = {}, o3 = {};
      {
        VFrag f0, f1;
        v_read<0>(f0, vb);
        pv_step<0>(o0, o1, o2, o3, f0, f1, vb, pa[0]);
        pv_step<1>(o0, o1, o2, o3, f1, f0, vb, pa[1]);
        pv_step<2>(o0, o1, o2, o3, f0, f1, vb, pa[2]);
        pv_step<3>(o0, o1, o2, o3, f1, f0, vb, pa[3]);
        pv_step<4>(o0, o1, o2, o3, f0, f1, vb, pa[4]);
        pv_step<5>(o0, o1, o2, o3, f1, f0, vb, pa[5]);
        pv_step<6>(o0, o1, o2, o3, f0, f1, vb, pa[6]);
        pv_step<7>(o0, o1, o2, o3, f1, f0, vb, pa[7]);
        pv_step<8>(o0, o1, o2, o3, f0, f1, vb, pa[8]);
        pv_step<9>(o0, o1, o2, o3, f1, f0, vb, pa[9]);
        pv_step<10>(o0, o1, o2, o3, f0, f1, vb, pa[10]);
        pv_step<11>(o0, o1, o2, o3, f1, f0, vb, pa[11]);
        pv_step<12>(o0, o1, o2, o3, f0, f1, vb, pa[12]);
        pv_step<13>(o0, o1, o2, o3, f1, f0, vb, pa[13]);
        pv_step<14>(o0, o1, o2, o3, f0, f1, vb, pa[14]);
        pv_step<15>(o0, o1, o2, o3, f1, f0, vb, pa[15]);
      }
      STAMP(4 + (item * 4 + g) * 4);
      const float rl = __builtin_amdgcn_rcpf(l);
      bf16* Ow = O + ((long)bk * G * SQ + g * SQ + wid * 32 + r32) * D;
#define STORE(OB, D0) do {                                                                                   \
      _Pragma("unroll") for (int gg = 0; gg < 4; gg += 2) {                                                  \
        unsigned ax = cvtpk(OB[4 * gg + 0] * rl, OB[4 * gg + 1] * rl), ay = cvtpk(OB[4 * gg + 2] * rl, OB[4 * gg + 3] * rl); \
        unsigned bx = cvtpk(OB[4 * gg + 4] * rl, OB[4 * gg + 5] * rl), by = cvtpk(OB[4 * gg + 6] * rl, OB[4 * gg + 7] * rl); \
        auto rx = __builtin_amdgcn_permlane32_swap(ax, bx, false, false);                                    \
        auto ry = __builtin_amdgcn_permlane32_swap(ay, by, false, false);                                    \
        u32x4 w = {rx[0], ry[0], rx[1], ry[1]};                                                              \
        *reinterpret_cast<u32x4*>(Ow + (D0) * 32 + gg * 8 + hi * 8) = w;                                     \
      } } while (0)
      STORE(o0, 0); STORE(o1, 1); STORE(o2, 2); STORE(o3, 3);
#undef STORE
      STAMP(5 + (item * 4 + g) * 4);
    }
    if (more) __syncthreads();
  }
}

extern "C" void kernel_launch(void* const* d_in, const int* in_sizes, int n_in,
                              void* d_out, int out_size, void* d_ws, size_t ws_size,
                              hipStream_t stream) {
  (void)in_sizes; (void)n_in; (void)out_size; (void)d_ws; (void)ws_size;
  hipFuncSetAttribute((const void*)gqa_attn_kernel, hipFuncAttributeMaxDynamicSharedMemorySize, 131072);
  hipLaunchKernelGGL(gqa_attn_kernel, dim3(256), dim3(512), 131072, stream,
                     (const bf16*)d_in[0], (const bf16*)d_in[1], (const bf16*)d_in[2], (bf16*)d_out, (unsigned long long*)d_ws);
}
```

```cpp
#include <hip/hip_runtime.h>
#include <hip/hip_bf16.h>

using bf16 = __hip_bfloat16;
constexpr int B_ = 64, HQ = 32, HKV = 8, SQ = 256, SK = 256, D = 128, G = HQ / HKV;
constexpr float SCALE = 0.088388347648318440f;

using bf16x8 = __attribute__((ext_vector_type(8))) short;
using s16x4  = __attribute__((ext_vector_type(4))) short;
using f32x16 = __attribute__((ext_vector_type(16))) float;
using u32x4  = __attribute__((ext_vector_type(4))) unsigned;

#define KSWZ(row, colB) ((row) * 256 + ((colB) ^ (((row) & 15) << 4)))
#define SBAR() __builtin_amdgcn_sched_barrier(0)

__device__ __forceinline__ unsigned cvtpk(float lo, float hi) {
  unsigned r; asm volatile("v_cvt_pk_bf16_f32 %0, %1, %2" : "=v"(r) : "v"(lo), "v"(hi)); return r;
}
__device__ __forceinline__ int v_st(int k, int c) {
  const int kk = (k & ~0xC) | ((k & 4) << 1) | ((k & 8) >> 1);
  return ((kk >> 3) * 4 + (c >> 5)) * 512 + ((kk & 7) * 32 + (c & 31)) * 2;
}
__device__ __forceinline__ int v_rd_base(int lane) {
  return ((lane & 3) << 3) | (((lane >> 2) & 3) << 6) | (((lane >> 4) & 1) << 5) | (((lane >> 5) & 1) << 8);
}
constexpr int v_rd_off(int d0, int ks, int half) { return d0 * 512 + ks * 4096 + half * 2048; }
template <int OFF> __device__ __forceinline__ s16x4 tr_read(int vb) {
  s16x4 r; asm volatile("ds_read_b64_tr_b16 %0, %1 offset:%2" : "=&v"(r) : "v"(vb), "i"(OFF) : "memory"); return r;
}

struct VFrag { s16x4 l[4], h[4]; };
template <int KS> __device__ __forceinline__ void v_read(VFrag& f, int vb) {
  f.l[0] = tr_read<v_rd_off(0, KS, 0)>(vb); f.h[0] = tr_read<v_rd_off(0, KS, 1)>(vb);
  f.l[1] = tr_read<v_rd_off(1, KS, 0)>(vb); f.h[1] = tr_read<v_rd_off(1, KS, 1)>(vb);
  f.l[2] = tr_read<v_rd_off(2, KS, 0)>(vb); f.h[2] = tr_read<v_rd_off(2, KS, 1)>(vb);
  f.l[3] = tr_read<v_rd_off(3, KS, 0)>(vb); f.h[3] = tr_read<v_rd_off(3, KS, 1)>(vb);
}
#define PK(L, H) (bf16x8){L[0], L[1], L[2], L[3], H[0], H[1], H[2], H[3]}
__device__ __forceinline__ void v_mma(f32x16& o0, f32x16& o1, f32x16& o2, f32x16& o3, const VFrag& f, bf16x8 p) {
  o0 = __builtin_amdgcn_mfma_f32_32x32x16_bf16(PK(f.l[0], f.h[0]), p, o0, 0, 0, 0);
  o1 = __builtin_amdgcn_mfma_f32_32x32x16_bf16(PK(f.l[1], f.h[1]), p, o1, 0, 0, 0);
  o2 = __builtin_amdgcn_mfma_f32_32x32x16_bf16(PK(f.l[2], f.h[2]), p, o2, 0, 0, 0);
  o3 = __builtin_amdgcn_mfma_f32_32x32x16_bf16(PK(f.l[3], f.h[3]), p, o3, 0, 0, 0);
  asm volatile("" : "+v"(o0), "+v"(o1), "+v"(o2), "+v"(o3));
}
template <int KS> __device__ __forceinline__ void pv_step(f32x16& o0, f32x16& o1, f32x16& o2, f32x16& o3,
                                                       VFrag& fa, VFrag& fb, int vb, bf16x8 p) {
  if constexpr (KS + 1 < 16) { v_read<KS + 1>(fb, vb); asm volatile("s_waitcnt lgkmcnt(8)" ::: "memory"); }
  else { asm volatile("s_waitcnt lgkmcnt(0)" ::: "memory"); }
  SBAR();
  v_mma(o0, o1, o2, o3, fa, p);
  SBAR();
}

__global__ __launch_bounds__(512, 2)
void gqa_attn_kernel(const bf16* __restrict__ Q, const bf16* __restrict__ K, const bf16* __restrict__ V,
                     bf16* __restrict__ O) {
  extern __shared__ __attribute__((aligned(16))) char lds[];
  char* K_lds = lds;
  char* V_lds = lds + 65536;
  const int tid = threadIdx.x, wid = tid >> 6, lane = tid & 63, r32 = lane & 31, hi = lane >> 5;
  const int sr = tid >> 4, sc = (tid & 15) * 8;
  const int vb = (int)(uintptr_t)V_lds + v_rd_base(lane);
  constexpr float C = SCALE * 1.4426950408889634f;
  constexpr int NITEM = 2;
  bf16x8 qr[8];
#define DMA_K(bk) do { int lane = tid & 63; asm volatile("" : "+v"(lane)); const bf16* Kh = K + (long)(bk) * SK * D;                                               \
    _Pragma("unroll") for (int i = 0; i < 8; ++i) { const int pc = wid * 8 + i, row = pc * 4 + (lane >> 4);     \
      __builtin_amdgcn_global_load_lds((const void*)(Kh + row * D + (((lane & 15) ^ (row & 15)) * 8)),          \
        (__attribute__((address_space(3))) void*)(K_lds + pc * 1024), 16, 0, 0); } } while (0)
#define DMA_V(bk) do { int lane = tid & 63; asm volatile("" : "+v"(lane)); const bf16* Vh = V + (long)(bk) * SK * D;                                               \
    _Pragma("unroll") for (int i = 0; i < 8; ++i) { const int pc = wid * 8 + i, st = pc * 2 + (lane >> 5);      \
      const int kk = (st >> 2) * 8 + ((lane & 31) >> 2), k = (kk & ~0xC) | ((kk & 4) << 1) | ((kk & 8) >> 1);   \
      __builtin_amdgcn_global_load_lds((const void*)(Vh + k * D + (st & 3) * 32 + (lane & 3) * 8),               \
        (__attribute__((address_space(3))) void*)(V_lds + pc * 1024), 16, 0, 0); } } while (0)
#define LOAD_Q(bk, g) do { const bf16* Qw = Q + ((long)(bk) * G * SQ + (g) * SQ + wid * 32 + r32) * D + hi * 8;  \
    _Pragma("unroll") for (int d0 = 0; d0 < 8; ++d0) qr[d0] = *reinterpret_cast<const bf16x8*>(Qw + d0 * 16); } while (0)

  const int bk0 = blockIdx.x;
  DMA_K(bk0); LOAD_Q(bk0, 0);
  __syncthreads();

  for (int item = 0; item < NITEM; ++item) {
    const int bk = bk0 + item * 256;
    const bool more = item + 1 < NITEM;
    DMA_V(bk);
#pragma unroll 1
    for (int g = 0; g < G; ++g) {
      f32x16 s[8];
#pragma unroll
      for (int kb = 0; kb < 8; ++kb) {
        s[kb] = f32x16{};
#pragma unroll
        for (int d0 = 0; d0 < 8; ++d0) {
          bf16x8 kf = *reinterpret_cast<const bf16x8*>(K_lds + KSWZ(kb * 32 + r32, (d0 * 16 + hi * 8) * 2));
          s[kb] = __builtin_amdgcn_mfma_f32_32x32x16_bf16(kf, qr[d0], s[kb], 0, 0, 0);
        }
      }
      if (g + 1 < G) { LOAD_Q(bk, g + 1); }
      else if (more) { LOAD_Q(bk + 256, 0); }
      float m = s[0][0];
#pragma unroll
      for (int kb = 0; kb < 8; ++kb)
#pragma unroll
        for (int r = 0; r < 16; ++r) m = fmaxf(m, s[kb][r]);
      { auto rr = __builtin_amdgcn_permlane32_swap(__float_as_uint(m), __float_as_uint(m), false, false);
        m = fmaxf(__uint_as_float(rr[0]), __uint_as_float(rr[1])); }
      const float mC = -m * C;
      float l = 0.f;
      bf16x8 pa[16];
#define PK4(P, BASE, OUT) do { unsigned a0 = cvtpk(P[BASE + 0], P[BASE + 1]), a1 = cvtpk(P[BASE + 2], P[BASE + 3]);   \
    unsigned b0 = cvtpk(P[BASE + 4], P[BASE + 5]), b1 = cvtpk(P[BASE + 6], P[BASE + 7]);                              \
    auto r0 = __builtin_amdgcn_permlane32_swap(a0, b0, false, false); auto r1 = __builtin_amdgcn_permlane32_swap(a1, b1, false, false); \
    u32x4 w = {r0[0], r1[0], r0[1], r1[1]}; OUT = *reinterpret_cast<bf16x8*>(&w); } while (0)
#pragma unroll
      for (int kb = 0; kb < 8; ++kb) {
        float pp[16];
#pragma unroll
        for (int r = 0; r < 16; ++r) { pp[r] = __builtin_amdgcn_exp2f(fmaf(s[kb][r], C, mC)); l += pp[r]; }
        PK4(pp, 0, pa[2 * kb]); PK4(pp, 8, pa[2 * kb + 1]);
        SBAR();
      }
#undef PK4
      { auto rr = __builtin_amdgcn_permlane32_swap(__float_as_uint(l), __float_as_uint(l), false, false);
        l = __uint_as_float(rr[0]) + __uint_as_float(rr[1]); }
      if (g == 0) { asm volatile("s_waitcnt vmcnt(0)" ::: "memory"); __syncthreads(); }
      if (g == G - 1 && more) { __syncthreads(); DMA_K(bk + 256); }
      f32x16 o0 = {}, o1 = {}, o2 = {}, o3 = {};
      {
        VFrag f0, f1;
        v_read<0>(f0, vb);
        pv_step<0>(o0, o1, o2, o3, f0, f1, vb, pa[0]);
        pv_step<1>(o0, o1, o2, o3, f1, f0, vb, pa[1]);
        pv_step<2>(o0, o1, o2, o3, f0, f1, vb, pa[2]);
        pv_step<3>(o0, o1, o2, o3, f1, f0, vb, pa[3]);
        pv_step<4>(o0, o1, o2, o3, f0, f1, vb, pa[4]);
        pv_step<5>(o0, o1, o2, o3, f1, f0, vb, pa[5]);
        pv_step<6>(o0, o1, o2, o3, f0, f1, vb, pa[6]);
        pv_step<7>(o0, o1, o2, o3, f1, f0, vb, pa[7]);
        pv_step<8>(o0, o1, o2, o3, f0, f1, vb, pa[8]);
        pv_step<9>(o0, o1, o2, o3, f1, f0, vb, pa[9]);
        pv_step<10>(o0, o1, o2, o3, f0, f1, vb, pa[10]);
        pv_step<11>(o0, o1, o2, o3, f1, f0, vb, pa[11]);
        pv_step<12>(o0, o1, o2, o3, f0, f1, vb, pa[12]);
        pv_step<13>(o0, o1, o2, o3, f1, f0, vb, pa[13]);
        pv_step<14>(o0, o1, o2, o3, f0, f1, vb, pa[14]);
        pv_step<15>(o0, o1, o2, o3, f1, f0, vb, pa[15]);
      }
      const float rl = __builtin_amdgcn_rcpf(l);
      bf16* Ow = O + ((long)bk * G * SQ + g * SQ + wid * 32 + r32) * D;
#define STORE(OB, D0) do {                                                                                   \
      _Pragma("unroll") for (int gg = 0; gg < 4; gg += 2) {                                                  \
        unsigned ax = cvtpk(OB[4 * gg + 0] * rl, OB[4 * gg + 1] * rl), ay = cvtpk(OB[4 * gg + 2] * rl, OB[4 * gg + 3] * rl); \
        unsigned bx = cvtpk(OB[4 * gg + 4] * rl, OB[4 * gg + 5] * rl), by = cvtpk(OB[4 * gg + 6] * rl, OB[4 * gg + 7] * rl); \
        auto rx = __builtin_amdgcn_permlane32_swap(ax, bx, false, false);                                    \
        auto ry = __builtin_amdgcn_permlane32_swap(ay, by, false, false);                                    \
        u32x4 w = {rx[0], ry[0], rx[1], ry[1]};                                                              \
        *reinterpret_cast<u32x4*>(Ow + (D0) * 32 + gg * 8 + hi * 8) = w;                                     \
      } } while (0)
      STORE(o0, 0); STORE(o1, 1); STORE(o2, 2); STORE(o3, 3);
#undef STORE
    }
    if (more) __syncthreads();
  }
}

extern "C" void kernel_launch(void* const* d_in, const int* in_sizes, int n_in,
                              void* d_out, int out_size, void* d_ws, size_t ws_size,
                              hipStream_t stream) {
  (void)in_sizes; (void)n_in; (void)out_size; (void)d_ws; (void)ws_size;
  hipFuncSetAttribute((const void*)gqa_attn_kernel, hipFuncAttributeMaxDynamicSharedMemorySize, 131072);
  hipLaunchKernelGGL(gqa_attn_kernel, dim3(256), dim3(512), 131072, stream,
                     (const bf16*)d_in[0], (const bf16*)d_in[1], (const bf16*)d_in[2], (bf16*)d_out);
}
```
